```python
import torch

def _softmax(x, axis=-1):
    return torch.softmax(x, dim=axis)
def _gelu(x):
    return torch.nn.functional.gelu(x, approximate="tanh")
def _silu(x):
    return torch.nn.functional.silu(x)
def _sigmoid(x):
    return torch.sigmoid(x)
def _layernorm(x, axis=-1, eps=1e-5):
    m = x.mean(dim=axis, keepdim=True)
    v = x.var(dim=axis, keepdim=True, unbiased=False)
    return (x - m) / torch.sqrt(v + eps)
def _scatter_add(tgt, idx, src, axis=0):
    return tgt.clone().index_add_(axis, idx, src)
def _segment_sum(src, seg, n):
    out = torch.zeros((n,) + src.shape[1:], dtype=src.dtype)
    return out.index_add_(0, seg, src)
def _pad(x, pw):
    flat = tuple(v for pair in reversed(pw) for v in pair)
    return torch.nn.functional.pad(x, flat)

kernel_name = 'sequential_preLN__mha_block__mixer_s_mlp__prompt'

def setup_inputs():

    _w = (lambda a: (lambda t: t.to(torch.bfloat16) if t.is_floating_point() else t)(torch.as_tensor(a)))
    def _A_setup():
        B, S, D, H = 1, 2048, 576, 32
        return {
            "x": _w((lambda *s: torch.randn(*s, dtype=torch.bfloat16) * 0.05)(B, S, D)),
            "wqkv": _w((lambda *s: torch.randn(*s, dtype=torch.bfloat16) * 0.05)(D, 3 * D)),
            "wo": _w((lambda *s: torch.randn(*s, dtype=torch.bfloat16) * 0.05)(D, D)),
        }
    _A_in = _A_setup()

    def _B_setup():
        B, S, D, F = 1, 2048, 576, 4608
        return {
            "x":  _w((lambda *s: torch.randn(*s, dtype=torch.bfloat16) * 0.05)(B, S, D)),
            "w1": _w((lambda *s: torch.randn(*s, dtype=torch.bfloat16) * 3.2)(D, D) * (D ** -0.5)),
            "w2": _w((lambda *s: torch.randn(*s, dtype=torch.bfloat16) * 3.2)(D, D) * (D ** -0.5)),
            "w3": _w((lambda *s: torch.randn(*s, dtype=torch.bfloat16) * 3.2)(D, F) * (D ** -0.5)),
            "w4": _w((lambda *s: torch.randn(*s, dtype=torch.bfloat16) * 3.2)(F, D) * (F ** -0.5)),
        }
    _B_in = _B_setup()

    return {
        "A_x": _A_in["x"],
        "A_wqkv": _A_in["wqkv"],
        "A_wo": _A_in["wo"],
        "B_x": _B_in["x"],
        "B_w1": _B_in["w1"],
        "B_w2": _B_in["w2"],
        "B_w3": _B_in["w3"],
        "B_w4": _B_in["w4"],
    }

def reference(A_x, A_wqkv, A_wo, B_x, B_w1, B_w2, B_w3, B_w4):
    def _A_ref(x, wqkv, wo):
        B, S, D = x.shape
        H = 32
        Dh = D // H
        qkv = (lambda x, *d: x.permute(*d))((x @ wqkv).reshape(B, S, 3, H, Dh), 2, 0, 3, 1, 4)
        q, k, v = qkv[0], qkv[1], qkv[2]
        scores = torch.einsum("bhqd,bhkd->bhqk", q, k) * (Dh ** -0.5)
        p = _softmax(scores, axis=-1)
        o = torch.einsum("bhqk,bhkd->bhqd", p, v)
        return (lambda x, *d: x.permute(*d))(o, 0, 2, 1, 3).reshape(B, S, D) @ wo

    def _B_ref(x, w1, w2, w3, w4):
        B, S, D = x.shape
        h = _gelu(_layernorm(x, axis=-1) @ w1)
        c = D // 4
        g0 = _pad(h[:, :, 0*c:1*c], ((0, 0), (1, 0), (0, 0)))[:, :S, :]
        g1 = _pad(h[:, :, 1*c:2*c], ((0, 0), (0, 1), (0, 0)))[:, 1:, :]
        g2 = _pad(h[:, :, 2*c:3*c], ((0, 0), (2, 0), (0, 0)))[:, :S, :]
        g3 = _pad(h[:, :, 3*c:   ], ((0, 0), (0, 2), (0, 0)))[:, 2:, :]
        hs = torch.concatenate([g0, g1, g2, g3], axis=-1) @ w2
        x1 = x + hs
        h2 = _gelu(_layernorm(x1, axis=-1) @ w3) @ w4
        return x1 + h2

    x = A_x
    h = x + _A_ref(_layernorm(x, axis=-1), A_wqkv, A_wo)
    h2 = h + _B_ref(_layernorm(h, axis=-1), B_w1, B_w2, B_w3, B_w4)
    return h2
```

```cpp
#include <hip/hip_runtime.h>
#include <hip/hip_bf16.h>

static constexpr int S_ = 2048;
static constexpr int D_ = 576;
static constexpr int H_ = 32;
static constexpr int DH = 18;
static constexpr int F_ = 4608;

typedef unsigned short u16;
typedef short bf16x8 __attribute__((ext_vector_type(8)));
typedef short s16x4 __attribute__((ext_vector_type(4)));
typedef float f32x4 __attribute__((ext_vector_type(4)));
typedef float f32x16 __attribute__((ext_vector_type(16)));
typedef unsigned u32x4 __attribute__((ext_vector_type(4)));
typedef unsigned u32x2 __attribute__((ext_vector_type(2)));
typedef __attribute__((address_space(3))) s16x4* lds_s16x4_ptr;

#define DI __device__ __forceinline__

DI float bf2f(u16 v) { return __uint_as_float(((unsigned)v) << 16); }
DI float bflo(unsigned u) { return __uint_as_float(u << 16); }
DI float bfhi(unsigned u) { return __uint_as_float(u & 0xffff0000u); }
DI u16 f2bf(float f) { return __builtin_bit_cast(u16, (__bf16)f); }
DI float rbf(float f) { return bf2f(f2bf(f)); }
DI unsigned pk2(float lo, float hi) { return (unsigned)f2bf(lo) | ((unsigned)f2bf(hi) << 16); }

DI float gelu_tanh(float x) {
  const float k0 = 0.7978845608028654f;
  float u = k0 * (x + 0.044715f * x * x * x);
  float e = __expf(-2.0f * u);
  return x / (1.0f + e);
}

DI float sum8(float v) {
  v += __shfl_xor(v, 1);
  v += __shfl_xor(v, 2);
  v += __shfl_xor(v, 4);
  return v;
}

DI float ln_elem(float x, float m, float s) { return rbf(rbf(x - m) / s); }

struct GemmArgs {
  const u16* A;
  const u16* W;
  u16* out;
  const u16* r1;
  const u16* r2;
  u16* aux;
  u16* aux2;
  u16* aux3;
  int K;
  int N;
};

template <int BN>
struct BStride {
  static constexpr int raw = BN * 2;
  static constexpr int t = raw / 32;
  static constexpr int value = (t & 1) ? raw : raw + 32;
};

template <int BM, int BN, int WM, int WN, int PRO, int EPI>
__global__ __launch_bounds__(256) void gemm_kernel(GemmArgs g) {
  constexpr int SB = BStride<BN>::value;
  constexpr int A_BYTES = BM * 128;
  constexpr int B_BYTES = 64 * SB;
  constexpr int BUF = A_BYTES + B_BYTES;
  constexpr int TM = BM / WM / 16;
  constexpr int TN = BN / WN / 16;
  constexpr int A_CH = BM * 8 / 256;
  constexpr int B_CHUNKS = 64 * (BN / 8);
  constexpr int B_CH = (B_CHUNKS + 255) / 256;
  static_assert(WM * WN == 4, "4 waves");
  static_assert(BM * 8 % 256 == 0, "A chunks");

  extern __shared__ __attribute__((aligned(16))) char smem[];

  const int t = threadIdx.x;
  const int lane = t & 63;
  const int wid = t >> 6;
  const int wm = wid / WN, wn = wid % WN;
  const int K = g.K, N = g.N;
  const int NK = K / 64;
  const int m0 = blockIdx.y * BM;
  const int n0 = blockIdx.x * BN;

  float mA[A_CH], sA[A_CH], mB[A_CH], sB_[A_CH];
  const int ac = t & 7;
  if constexpr (PRO == 1 || PRO == 2) {
#pragma unroll
    for (int i = 0; i < A_CH; ++i) {
      int row = (t >> 3) + 32 * i;
      const u16* rp = g.A + (size_t)(m0 + row) * D_ + ac * 8;
      u32x4 v[9];
#pragma unroll
      for (int kb = 0; kb < 9; ++kb) v[kb] = *(const u32x4*)(rp + kb * 64);
      float s = 0.f;
#pragma unroll
      for (int kb = 0; kb < 9; ++kb)
#pragma unroll
        for (int e = 0; e < 4; ++e) s += bflo(v[kb][e]) + bfhi(v[kb][e]);
      s = sum8(s);
      float mean = s * (1.0f / 576.0f);
      float q = 0.f;
#pragma unroll
      for (int kb = 0; kb < 9; ++kb)
#pragma unroll
        for (int e = 0; e < 4; ++e) {
          float a = bflo(v[kb][e]) - mean, b = bfhi(v[kb][e]) - mean;
          q += a * a + b * b;
        }
      q = sum8(q);
      float var = q * (1.0f / 576.0f);
      float m1 = rbf(mean);
      float s1 = rbf(sqrtf(rbf(rbf(var) + 1e-5f)));
      mA[i] = m1;
      sA[i] = s1;
      if constexpr (PRO == 2) {
        float s2 = 0.f;
#pragma unroll
        for (int kb = 0; kb < 9; ++kb) {
          u32x4 y;
#pragma unroll
          for (int e = 0; e < 4; ++e) {
            float a = ln_elem(bflo(v[kb][e]), m1, s1), b = ln_elem(bfhi(v[kb][e]), m1, s1);
            s2 += a + b;
            y[e] = pk2(a, b);
          }
          v[kb] = y;
        }
        if (blockIdx.x == 0) {
          u16* xp = g.aux + (size_t)(m0 + row) * D_ + ac * 8;
#pragma unroll
          for (int kb = 0; kb < 9; ++kb) *(u32x4*)(xp + kb * 64) = v[kb];
        }
        s2 = sum8(s2);
        float mean2 = s2 * (1.0f / 576.0f);
        float q2 = 0.f;
#pragma unroll
        for (int kb = 0; kb < 9; ++kb)
#pragma unroll
          for (int e = 0; e < 4; ++e) {
            float a = bflo(v[kb][e]) - mean2, b = bfhi(v[kb][e]) - mean2;
            q2 += a * a + b * b;
          }
        q2 = sum8(q2);
        float var2 = q2 * (1.0f / 576.0f);
        mB[i] = rbf(mean2);
        sB_[i] = rbf(sqrtf(rbf(rbf(var2) + 1e-5f)));
      }
    }
  }

  u32x4 ra[A_CH];
  u32x4 rb[B_CH];

  auto load_tile = [&](int kb) {
#pragma unroll
    for (int i = 0; i < A_CH; ++i) {
      int row = (t >> 3) + 32 * i;
      int k = kb * 64 + ac * 8;
      if constexpr (PRO == 3) {
        int grp = k / 144;
        int sh = (grp == 0) ? -1 : (grp == 1) ? 1 : (grp == 2) ? -2 : 2;
        int sr = m0 + row + sh;
        if (sr >= 0 && sr < S_) ra[i] = *(const u32x4*)(g.A + (size_t)sr * K + k);
        else ra[i] = u32x4{0u, 0u, 0u, 0u};
      } else {
        ra[i] = *(const u32x4*)(g.A + (size_t)(m0 + row) * K + k);
      }
    }
#pragma unroll
    for (int i = 0; i < B_CH; ++i) {
      int id = t + 256 * i;
      if (B_CHUNKS % 256 == 0 || id < B_CHUNKS) {
        int kr = id / (BN / 8), c = id % (BN / 8);
        rb[i] = *(const u32x4*)(g.W + (size_t)(kb * 64 + kr) * N + n0 + c * 8);
      }
    }
  };

  auto store_tile = [&](char* buf) {
    char* As = buf;
    char* Bs = buf + A_BYTES;
#pragma unroll
    for (int i = 0; i < A_CH; ++i) {
      int row = (t >> 3) + 32 * i;
      u32x4 v = ra[i];
      if constexpr (PRO == 1 || PRO == 2) {
#pragma unroll
        for (int e = 0; e < 4; ++e) {
          float a = ln_elem(bflo(v[e]), mA[i], sA[i]), b = ln_elem(bfhi(v[e]), mA[i], sA[i]);
          if constexpr (PRO == 2) {
            a = ln_elem(a, mB[i], sB_[i]);
            b = ln_elem(b, mB[i], sB_[i]);
          }
          v[e] = pk2(a, b);
        }
      }
      int step = ac >> 2, cc = ac & 3;
      int sw = (row >> 1) & 7;
      int slot_lo = step * 4 + ((2 * cc) & 3);
      int slot_hi = step * 4 + ((2 * cc + 1) & 3);
      int sub = (cc >> 1) * 8;
      *(u32x2*)(As + row * 128 + ((slot_lo ^ sw) * 16) + sub) = u32x2{v[0], v[1]};
      *(u32x2*)(As + row * 128 + ((slot_hi ^ sw) * 16) + sub) = u32x2{v[2], v[3]};
    }
#pragma unroll
    for (int i = 0; i < B_CH; ++i) {
      int id = t + 256 * i;
      if (B_CHUNKS % 256 == 0 || id < B_CHUNKS) {
        int kr = id / (BN / 8), c = id % (BN / 8);
        *(u32x4*)(Bs + kr * SB + c * 16) = rb[i];
      }
    }
  };

  f32x4 acc[TM][TN];
#pragma unroll
  for (int i = 0; i < TM; ++i)
#pragma unroll
    for (int j = 0; j < TN; ++j) acc[i][j] = f32x4{0.f, 0.f, 0.f, 0.f};

  const int fr = lane & 15, fg = lane >> 4;
  const int tq = fr >> 2, tp = fr & 3;

  auto compute = [&](const char* buf) {
    const char* As = buf;
    const char* Bs = buf + A_BYTES;
#pragma unroll
    for (int st = 0; st < 2; ++st) {
      bf16x8 af[TM], bfr[TN];
#pragma unroll
      for (int i = 0; i < TM; ++i) {
        int row = wm * (BM / WM) + i * 16 + fr;
        int slot = (st * 4 + fg) ^ ((row >> 1) & 7);
        af[i] = *(const bf16x8*)(As + row * 128 + slot * 16);
      }
#pragma unroll
      for (int j = 0; j < TN; ++j) {
        int col = wn * (BN / WN) + j * 16 + tp * 4;
        const char* p0 = Bs + (st * 32 + fg * 4 + tq) * SB + col * 2;
        s16x4 lo = __builtin_amdgcn_ds_read_tr16_b64_v4i16((lds_s16x4_ptr)(p0));
        s16x4 hi = __builtin_amdgcn_ds_read_tr16_b64_v4i16((lds_s16x4_ptr)(p0 + 16 * SB));
        bfr[j] = __builtin_shufflevector(lo, hi, 0, 1, 2, 3, 4, 5, 6, 7);
      }
#pragma unroll
      for (int i = 0; i < TM; ++i)
#pragma unroll
        for (int j = 0; j < TN; ++j)
          acc[i][j] = __builtin_amdgcn_mfma_f32_16x16x32_bf16(af[i], bfr[j], acc[i][j], 0, 0, 0);
    }
  };

  load_tile(0);
  store_tile(smem);
  __syncthreads();
  for (int kb = 0; kb < NK; ++kb) {
    char* cur = smem + (kb & 1) * BUF;
    char* nxt = smem + ((kb + 1) & 1) * BUF;
    if (kb + 1 < NK) load_tile(kb + 1);
    compute(cur);
    if (kb + 1 < NK) store_tile(nxt);
    __syncthreads();
  }

  if constexpr (EPI == 0) {
#pragma unroll
    for (int i = 0; i < TM; ++i)
#pragma unroll
      for (int j = 0; j < TN; ++j) {
        int n = n0 + wn * (BN / WN) + j * 16 + fr;
        int which = n / D_;
        int rem = n - which * D_;
        int hh = rem / DH;
        int d = rem - hh * DH;
        int s = m0 + wm * (BM / WM) + i * 16 + fg * 4;
        if (which == 2) {
          int u = (s >> 2) & 3;
          int pos = (s & ~15) + ((((u & 1) << 1) | (u >> 1)) << 2);
          u32x2 v{pk2(acc[i][j][0], acc[i][j][1]), pk2(acc[i][j][2], acc[i][j][3])};
          *(u32x2*)(g.aux3 + (size_t)(hh * 32 + d) * S_ + pos) = v;
        } else {
          u16* dst = (which == 0 ? g.aux : g.aux2) + ((size_t)hh * S_ + s) * 32 + d;
#pragma unroll
          for (int e = 0; e < 4; ++e) dst[e * 32] = f2bf(acc[i][j][e]);
        }
      }
  } else {
    constexpr int CS = BN + 4;
    float* Cs = (float*)smem;
#pragma unroll
    for (int i = 0; i < TM; ++i)
#pragma unroll
      for (int j = 0; j < TN; ++j) {
        int col = wn * (BN / WN) + j * 16 + fr;
        int row = wm * (BM / WM) + i * 16 + fg * 4;
#pragma unroll
        for (int e = 0; e < 4; ++e) Cs[(row + e) * CS + col] = acc[i][j][e];
      }
    __syncthreads();
    constexpr int CH = BM * (BN / 8);
#pragma unroll
    for (int it = 0; it < (CH + 255) / 256; ++it) {
      int id = t + 256 * it;
      if (CH % 256 == 0 || id < CH) {
        int row = id / (BN / 8), c = id % (BN / 8);
        f32x4 c0 = *(const f32x4*)(Cs + row * CS + c * 8);
        f32x4 c1 = *(const f32x4*)(Cs + row * CS + c * 8 + 4);
        float v[8] = {c0[0], c0[1], c0[2], c0[3], c1[0], c1[1], c1[2], c1[3]};
        size_t go = (size_t)(m0 + row) * N + n0 + c * 8;
        u32x4 o;
        if constexpr (EPI == 1 || EPI == 3) {
          u32x4 r = *(const u32x4*)(g.r1 + go);
#pragma unroll
          for (int e = 0; e < 4; ++e)
            o[e] = pk2(rbf(v[2 * e]) + bflo(r[e]), rbf(v[2 * e + 1]) + bfhi(r[e]));
        } else if constexpr (EPI == 2 || EPI == 4) {
#pragma unroll
          for (int e = 0; e < 4; ++e)
            o[e] = pk2(gelu_tanh(rbf(v[2 * e])), gelu_tanh(rbf(v[2 * e + 1])));
        } else {
          u32x4 r = *(const u32x4*)(g.r1 + go);
          u32x4 r2 = *(const u32x4*)(g.r2 + go);
#pragma unroll
          for (int e = 0; e < 4; ++e) {
            float a = rbf(rbf(v[2 * e]) + bflo(r[e])) + bflo(r2[e]);
            float b = rbf(rbf(v[2 * e + 1]) + bfhi(r[e])) + bfhi(r2[e]);
            o[e] = pk2(a, b);
          }
        }
        *(u32x4*)(g.out + go) = o;
      }
    }
  }
}

__global__ __launch_bounds__(256) void attn_kernel(const u16* __restrict__ Qp, const u16* __restrict__ Kp,
                                                   const u16* __restrict__ Vt, u16* __restrict__ O) {
  __shared__ __attribute__((aligned(16))) char smem[2 * 8192];
  const int t = threadIdx.x;
  const int lane = t & 63, wid = t >> 6;
  const int r = lane & 31, h = lane >> 5;
  const int head = blockIdx.y;
  const int q0 = blockIdx.x * 128 + wid * 32;

  bf16x8 qf[2];
  {
    const u16* qp = Qp + ((size_t)head * S_ + q0 + r) * 32;
    u32x4 a = *(const u32x4*)(qp + 8 * h);
    u32x4 b = *(const u32x4*)(qp + 16 + 8 * h);
    if (h == 0) { b[1] = 0; b[2] = 0; b[3] = 0; }
    else { b = u32x4{0u, 0u, 0u, 0u}; }
    qf[0] = __builtin_bit_cast(bf16x8, a);
    qf[1] = __builtin_bit_cast(bf16x8, b);
  }

  const int kkey = t >> 2, kc = t & 3;
  const int vd = t >> 3, vc = t & 7;
  const u16* kbase = Kp + ((size_t)head * S_ + kkey) * 32 + kc * 8;
  const u16* vbase = Vt + ((size_t)head * 32 + vd) * S_ + vc * 8;
  const int kdst = kkey * 64 + ((kc ^ ((kkey >> 2) & 3)) * 16);
  const int vdst = 4096 + vd * 128 + ((vc ^ ((vd >> 1) & 7)) * 16);

  u32x4 rk, rv;
  auto load_tile = [&](int kt) {
    rk = *(const u32x4*)(kbase + (size_t)kt * 64 * 32);
    if (kc == 2) { rk[1] = 0; rk[2] = 0; rk[3] = 0; }
    if (kc == 3) rk = u32x4{0u, 0u, 0u, 0u};
    if (vd < DH) rv = *(const u32x4*)(vbase + kt * 64);
    else if (vd == DH) rv = u32x4{0x3f803f80u, 0x3f803f80u, 0x3f803f80u, 0x3f803f80u};
    else rv = u32x4{0u, 0u, 0u, 0u};
  };
  auto store_tile = [&](char* buf) {
    *(u32x4*)(buf + kdst) = rk;
    *(u32x4*)(buf + vdst) = rv;
  };

  f32x16 o;
#pragma unroll
  for (int i = 0; i < 16; ++i) o[i] = 0.f;
  float m = -1e30f;
  const float c = 0.23570226039551584f * 1.4426950408889634f;

  load_tile(0);
  store_tile(smem);
  __syncthreads();
  constexpr int NT = S_ / 64;
  for (int kt = 0; kt < NT; ++kt) {
    const char* buf = smem + (kt & 1) * 8192;
    if (kt + 1 < NT) load_tile(kt + 1);
    f32x16 s[2];
#pragma unroll
    for (int sub = 0; sub < 2; ++sub) {
#pragma unroll
      for (int i = 0; i < 16; ++i) s[sub][i] = 0.f;
      int key = sub * 32 + r;
#pragma unroll
      for (int st = 0; st < 2; ++st) {
        int ch = (2 * st + h) ^ ((key >> 2) & 3);
        bf16x8 kf = *(const bf16x8*)(buf + key * 64 + ch * 16);
        s[sub] = __builtin_amdgcn_mfma_f32_32x32x16_bf16(kf, qf[st], s[sub], 0, 0, 0);
      }
    }
    float tm = s[0][0];
#pragma unroll
    for (int i = 1; i < 16; ++i) tm = fmaxf(tm, s[0][i]);
#pragma unroll
    for (int i = 0; i < 16; ++i) tm = fmaxf(tm, s[1][i]);
    tm = fmaxf(tm, __shfl_xor(tm, 32));
    float mn = fmaxf(m, tm);
    float alpha = exp2f((m - mn) * c);
    m = mn;
    float mc = mn * c;
#pragma unroll
    for (int i = 0; i < 16; ++i) o[i] *= alpha;
#pragma unroll
    for (int sub = 0; sub < 2; ++sub) {
#pragma unroll
      for (int ks = 0; ks < 2; ++ks) {
        u32x4 p;
#pragma unroll
        for (int e = 0; e < 4; ++e) {
          float a = exp2f(fmaf(s[sub][8 * ks + 2 * e], c, -mc));
          float b = exp2f(fmaf(s[sub][8 * ks + 2 * e + 1], c, -mc));
          p[e] = pk2(a, b);
        }
        int ch = (2 * (2 * sub + ks) + h) ^ ((r >> 1) & 7);
        bf16x8 vf = *(const bf16x8*)(buf + 4096 + r * 128 + ch * 16);
        o = __builtin_amdgcn_mfma_f32_32x32x16_bf16(vf, __builtin_bit_cast(bf16x8, p), o, 0, 0, 0);
      }
    }
    if (kt + 1 < NT) store_tile(smem + ((kt + 1) & 1) * 8192);
    __syncthreads();
  }
  float l = __shfl(o[10], r);
  float inv = 1.0f / l;
  u16* op = O + (size_t)(q0 + r) * D_ + head * DH;
#pragma unroll
  for (int gq = 0; gq < 3; ++gq) {
    int d0 = 8 * gq + 4 * h;
#pragma unroll
    for (int e = 0; e < 4; ++e) {
      int d = d0 + e;
      if (d < DH) op[d] = f2bf(o[4 * gq + e] * inv);
    }
  }
}

extern "C" void kernel_launch(void* const* d_in, const int* in_sizes, int n_in, void* d_out, int out_size,
                              void* d_ws, size_t ws_size, hipStream_t stream) {
  const u16* x = (const u16*)d_in[0];
  const u16* wqkv = (const u16*)d_in[1];
  const u16* wo = (const u16*)d_in[2];
  const u16* w1 = (const u16*)d_in[4];
  const u16* w2 = (const u16*)d_in[5];
  const u16* w3 = (const u16*)d_in[6];
  const u16* w4 = (const u16*)d_in[7];
  u16* out = (u16*)d_out;

  char* ws = (char*)d_ws;
  size_t off = 0;
  auto alloc = [&](size_t bytes) { char* p = ws + off; off += (bytes + 255) & ~(size_t)255; return p; };
  u16* Qp = (u16*)alloc((size_t)H_ * S_ * 32 * 2);
  u16* Kp = (u16*)alloc((size_t)H_ * S_ * 32 * 2);
  u16* Vt = (u16*)alloc((size_t)H_ * 32 * S_ * 2);
  u16* Ob = (u16*)alloc((size_t)S_ * D_ * 2);
  u16* hb = (u16*)alloc((size_t)S_ * D_ * 2);
  u16* xB = (u16*)alloc((size_t)S_ * D_ * 2);
  u16* hB = (u16*)alloc((size_t)S_ * D_ * 2);
  u16* x1 = (u16*)alloc((size_t)S_ * D_ * 2);
  u16* gb = (u16*)alloc((size_t)S_ * F_ * 2);

  auto smem_bytes = [](int BM, int BN) {
    int raw = BN * 2; int SB = ((raw / 32) & 1) ? raw : raw + 32;
    int buf = BM * 128 + 64 * SB;
    int c = BM * (BN + 4) * 4;
    return (2 * buf > c) ? 2 * buf : c;
  };

  GemmArgs a{};
  a = GemmArgs{x, wqkv, nullptr, nullptr, nullptr, Qp, Kp, Vt, D_, 3 * D_};
  gemm_kernel<64, 96, 2, 2, 1, 0><<<dim3(3 * D_ / 96, S_ / 64), 256, smem_bytes(64, 96), stream>>>(a);
  attn_kernel<<<dim3(S_ / 128, H_), 256, 0, stream>>>(Qp, Kp, Vt, Ob);
  a = GemmArgs{Ob, wo, hb, x, nullptr, nullptr, nullptr, nullptr, D_, D_};
  gemm_kernel<64, 96, 2, 2, 0, 1><<<dim3(D_ / 96, S_ / 64), 256, smem_bytes(64, 96), stream>>>(a);
  a = GemmArgs{hb, w1, hB, nullptr, nullptr, xB, nullptr, nullptr, D_, D_};
  gemm_kernel<64, 96, 2, 2, 2, 2><<<dim3(D_ / 96, S_ / 64), 256, smem_bytes(64, 96), stream>>>(a);
  a = GemmArgs{hB, w2, x1, xB, nullptr, nullptr, nullptr, nullptr, D_, D_};
  gemm_kernel<64, 96, 2, 2, 3, 3><<<dim3(D_ / 96, S_ / 64), 256, smem_bytes(64, 96), stream>>>(a);
  a = GemmArgs{x1, w3, gb, nullptr, nullptr, nullptr, nullptr, nullptr, D_, F_};
  gemm_kernel<128, 96, 2, 2, 1, 4><<<dim3(F_ / 96, S_ / 128), 256, smem_bytes(128, 96), stream>>>(a);
  a = GemmArgs{gb, w4, out, x1, hb, nullptr, nullptr, nullptr, F_, D_};
  gemm_kernel<64, 96, 2, 2, 0, 5><<<dim3(D_ / 96, S_ / 64), 256, smem_bytes(64, 96), stream>>>(a);
}
```

```cpp
#include <hip/hip_runtime.h>
#include <hip/hip_bf16.h>

static constexpr int S_ = 2048;
static constexpr int D_ = 576;
static constexpr int H_ = 32;
static constexpr int DH = 18;
static constexpr int F_ = 4608;

typedef unsigned short u16;
typedef short bf16x8 __attribute__((ext_vector_type(8)));
typedef short s16x4 __attribute__((ext_vector_type(4)));
typedef float f32x4 __attribute__((ext_vector_type(4)));
typedef float f32x16 __attribute__((ext_vector_type(16)));
typedef unsigned u32x4 __attribute__((ext_vector_type(4)));
typedef unsigned u32x2 __attribute__((ext_vector_type(2)));
typedef __attribute__((address_space(3))) s16x4* lds_s16x4_ptr;

#define DI __device__ __forceinline__

DI float bf2f(u16 v) { return __uint_as_float(((unsigned)v) << 16); }
DI float bflo(unsigned u) { return __uint_as_float(u << 16); }
DI float bfhi(unsigned u) { return __uint_as_float(u & 0xffff0000u); }
DI u16 f2bf(float f) { return __builtin_bit_cast(u16, (__bf16)f); }
DI float rbf(float f) { return bf2f(f2bf(f)); }
typedef __bf16 bf16x2_t __attribute__((ext_vector_type(2)));
typedef float f32x2_t __attribute__((ext_vector_type(2)));
DI unsigned pk2(float lo, float hi) {
  f32x2_t v{lo, hi};
  return __builtin_bit_cast(unsigned, __builtin_convertvector(v, bf16x2_t));
}

DI float gelu_tanh(float x) {
  const float k0 = 0.7978845608028654f;
  float u = k0 * (x + 0.044715f * x * x * x);
  float e = __builtin_amdgcn_exp2f(-2.8853900817779268f * u);
  return x * __builtin_amdgcn_rcpf(1.0f + e);
}

DI float sum8(float v) {
  v += __shfl_xor(v, 1);
  v += __shfl_xor(v, 2);
  v += __shfl_xor(v, 4);
  return v;
}

DI float ln_elem(float x, float m, float s) { return rbf(rbf(x - m) / s); }

struct GemmArgs {
  const u16* A;
  const u16* W;
  u16* out;
  const u16* r1;
  const u16* r2;
  u16* aux;
  u16* aux2;
  u16* aux3;
  int K;
  int N;
};

template <int BN>
struct BStride {
  static constexpr int raw = BN * 2;
  static constexpr int t = raw / 32;
  static constexpr int value = (t & 1) ? raw : raw + 32;
};

template <int BM, int BN, int WM, int WN, int PRO, int EPI, int NKC, int PD>
__global__ __launch_bounds__(WM* WN * 64, (WM * WN == 8 && PRO != 2 && BN == 96) ? 4 : 2) void gemm_kernel(GemmArgs g) {
  constexpr int NT = WM * WN * 64;
  constexpr int SB = BStride<BN>::value;
  constexpr int A_BYTES = BM * 128;
  constexpr int B_BYTES = 64 * SB;
  constexpr int BUF = A_BYTES + B_BYTES;
  constexpr int TM = BM / WM / 16;
  constexpr int TN = BN / WN / 16;
  constexpr int A_CH = BM * 8 / NT;
  constexpr int RSTEP = NT / 8;
  constexpr int B_CHUNKS = 64 * (BN / 8);
  constexpr int B_CH = (B_CHUNKS + NT - 1) / NT;
  static_assert(BM * 8 % NT == 0, "A chunks");

  extern __shared__ __attribute__((aligned(16))) char smem[];

  const int t = threadIdx.x;
  const int lane = t & 63;
  const int wid = t >> 6;
  const int wm = wid / WN, wn = wid % WN;
  const int K = g.K, N = g.N;
  const int NK = K / 64;
  const int m0 = blockIdx.y * BM;
  const int n0 = blockIdx.x * BN;
  const int ac = t & 7;

  constexpr int PDB = (NKC == 9) ? ((PRO == 1) ? 2 : 3) : PD;
  u32x4 ra[PD][A_CH];
  u32x4 rb[PDB][B_CH];

  auto load_a = [&](int kb, int slot) {
#pragma unroll
    for (int i = 0; i < A_CH; ++i) {
      int row = (t >> 3) + RSTEP * i;
      int k = kb * 64 + ac * 8;
      if constexpr (PRO == 3) {
        int grp = k / 144;
        int sh = (grp == 0) ? -1 : (grp == 1) ? 1 : (grp == 2) ? -2 : 2;
        int sr = m0 + row + sh;
        if (sr >= 0 && sr < S_) ra[slot][i] = *(const u32x4*)(g.A + (size_t)sr * K + k);
        else ra[slot][i] = u32x4{0u, 0u, 0u, 0u};
      } else {
        ra[slot][i] = *(const u32x4*)(g.A + (size_t)(m0 + row) * K + k);
      }
    }
  };
  auto load_b = [&](int kb, int slot) {
#pragma unroll
    for (int i = 0; i < B_CH; ++i) {
      int id = t + NT * i;
      if (B_CHUNKS % NT == 0 || id < B_CHUNKS) {
        int kr = id / (BN / 8), c = id % (BN / 8);
        rb[slot][i] = *(const u32x4*)(g.W + (size_t)(kb * 64 + kr) * N + n0 + c * 8);
      }
    }
  };

  float mA[A_CH], iA[A_CH], mB[A_CH], iB[A_CH];

  auto store_tile = [&](char* buf, int slot, int slotb) {
    char* As = buf;
    char* Bs = buf + A_BYTES;
#pragma unroll
    for (int i = 0; i < A_CH; ++i) {
      int row = (t >> 3) + RSTEP * i;
      u32x4 v = ra[slot][i];
      if constexpr (PRO == 1 || PRO == 2) {
#pragma unroll
        for (int e = 0; e < 4; ++e) {
          float a = rbf(rbf(bflo(v[e]) - mA[i]) * iA[i]), b = rbf(rbf(bfhi(v[e]) - mA[i]) * iA[i]);
          if constexpr (PRO == 2) {
            a = rbf(a - mB[i]) * iB[i];
            b = rbf(b - mB[i]) * iB[i];
          }
          v[e] = pk2(a, b);
        }
      }
      int step = ac >> 2, cc = ac & 3;
      int sw = (row >> 1) & 7;
      int slot_lo = step * 4 + ((2 * cc) & 3);
      int slot_hi = step * 4 + ((2 * cc + 1) & 3);
      int sub = (cc >> 1) * 8;
      *(u32x2*)(As + row * 128 + ((slot_lo ^ sw) * 16) + sub) = u32x2{v[0], v[1]};
      *(u32x2*)(As + row * 128 + ((slot_hi ^ sw) * 16) + sub) = u32x2{v[2], v[3]};
    }
#pragma unroll
    for (int i = 0; i < B_CH; ++i) {
      int id = t + NT * i;
      if (B_CHUNKS % NT == 0 || id < B_CHUNKS) {
        int kr = id / (BN / 8), c = id % (BN / 8);
        *(u32x4*)(Bs + kr * SB + c * 16) = rb[slotb][i];
      }
    }
  };

  f32x4 acc[TM][TN];
#pragma unroll
  for (int i = 0; i < TM; ++i)
#pragma unroll
    for (int j = 0; j < TN; ++j) acc[i][j] = f32x4{0.f, 0.f, 0.f, 0.f};

  const int fr = lane & 15, fg = lane >> 4;
  const int tq = fr >> 2, tp = fr & 3;

  auto compute = [&](const char* buf) {
    const char* As = buf;
    const char* Bs = buf + A_BYTES;
#pragma unroll
    for (int st = 0; st < 2; ++st) {
      bf16x8 af[TM], bfr[TN];
#pragma unroll
      for (int i = 0; i < TM; ++i) {
        int row = wm * (BM / WM) + i * 16 + fr;
        int slot = (st * 4 + fg) ^ ((row >> 1) & 7);
        af[i] = *(const bf16x8*)(As + row * 128 + slot * 16);
      }
#pragma unroll
      for (int j = 0; j < TN; ++j) {
        int col = wn * (BN / WN) + j * 16 + tp * 4;
        const char* p0 = Bs + (st * 32 + fg * 4 + tq) * SB + col * 2;
        s16x4 lo = __builtin_amdgcn_ds_read_tr16_b64_v4i16((lds_s16x4_ptr)(p0));
        s16x4 hi = __builtin_amdgcn_ds_read_tr16_b64_v4i16((lds_s16x4_ptr)(p0 + 16 * SB));
        bfr[j] = __builtin_shufflevector(lo, hi, 0, 1, 2, 3, 4, 5, 6, 7);
      }
#pragma unroll
      for (int i = 0; i < TM; ++i)
#pragma unroll
        for (int j = 0; j < TN; ++j)
          acc[i][j] = __builtin_amdgcn_mfma_f32_16x16x32_bf16(af[i], bfr[j], acc[i][j], 0, 0, 0);
    }
  };

  if constexpr (NKC == 9) {
    static_assert(PD == 9, "pd");
#pragma unroll
    for (int kb = 0; kb < 9; ++kb) load_a(kb, kb);
#pragma unroll
    for (int kb = 0; kb < PDB; ++kb) load_b(kb, kb);
    if constexpr (PRO == 1 || PRO == 2) {
#pragma unroll
      for (int i = 0; i < A_CH; ++i) {
        float s = 0.f;
#pragma unroll
        for (int kb = 0; kb < 9; ++kb)
#pragma unroll
          for (int e = 0; e < 4; ++e) s += bflo(ra[kb][i][e]) + bfhi(ra[kb][i][e]);
        s = sum8(s);
        float mean = s * (1.0f / 576.0f);
        float q = 0.f;
#pragma unroll
        for (int kb = 0; kb < 9; ++kb)
#pragma unroll
          for (int e = 0; e < 4; ++e) {
            float a = bflo(ra[kb][i][e]) - mean, b = bfhi(ra[kb][i][e]) - mean;
            q += a * a + b * b;
          }
        q = sum8(q);
        float var = q * (1.0f / 576.0f);
        float m1 = rbf(mean);
        float i1 = 1.0f / rbf(sqrtf(rbf(rbf(var) + 1e-5f)));
        mA[i] = m1;
        iA[i] = i1;
        if constexpr (PRO == 2) {
          float s2 = 0.f;
          const bool wr = (blockIdx.x == 0);
          u16* xp = g.aux + (size_t)(m0 + (t >> 3) + RSTEP * i) * D_ + ac * 8;
#pragma unroll
          for (int kb = 0; kb < 9; ++kb) {
            u32x4 y;
#pragma unroll
            for (int e = 0; e < 4; ++e) {
              float a = rbf(rbf(bflo(ra[kb][i][e]) - m1) * i1), b = rbf(rbf(bfhi(ra[kb][i][e]) - m1) * i1);
              s2 += a + b;
              y[e] = pk2(a, b);
            }
            if (wr) *(u32x4*)(xp + kb * 64) = y;
          }
          s2 = sum8(s2);
          float mean2 = s2 * (1.0f / 576.0f);
          float q2 = 0.f;
#pragma unroll
          for (int kb = 0; kb < 9; ++kb)
#pragma unroll
            for (int e = 0; e < 4; ++e) {
              float a = rbf(rbf(bflo(ra[kb][i][e]) - m1) * i1) - mean2;
              float b = rbf(rbf(bfhi(ra[kb][i][e]) - m1) * i1) - mean2;
              q2 += a * a + b * b;
            }
          q2 = sum8(q2);
          float var2 = q2 * (1.0f / 576.0f);
          mB[i] = rbf(mean2);
          iB[i] = 1.0f / rbf(sqrtf(rbf(rbf(var2) + 1e-5f)));
        }
      }
    }
    store_tile(smem, 0, 0);
    load_b(PDB, 0);
    __syncthreads();
#pragma unroll
    for (int kb = 0; kb < 9; ++kb) {
      if (kb + 1 < 9) {
        store_tile(smem + ((kb + 1) & 1) * BUF, kb + 1, (kb + 1) % PDB);
        if (kb + 1 + PDB < 9) load_b(kb + 1 + PDB, (kb + 1) % PDB);
      }
      compute(smem + (kb & 1) * BUF);
      __syncthreads();
    }
  } else {
    static_assert(PRO == 0, "ring path: plain A only");
#pragma unroll
    for (int p = 0; p < PD; ++p) { load_a(p, p); load_b(p, p); }
    store_tile(smem, 0, 0);
    load_a(PD, 0); load_b(PD, 0);
    __syncthreads();
    for (int kb0 = 0; kb0 < NK; kb0 += PD) {
#pragma unroll
      for (int p = 0; p < PD; ++p) {
        int kb = kb0 + p;
        if (kb + 1 < NK) {
          store_tile(smem + ((kb + 1) & 1) * BUF, (p + 1) % PD, (p + 1) % PD);
          if (kb + 1 + PD < NK) { load_a(kb + 1 + PD, (p + 1) % PD); load_b(kb + 1 + PD, (p + 1) % PD); }
        }
        compute(smem + (kb & 1) * BUF);
        __syncthreads();
      }
    }
  }

  if constexpr (EPI == 0) {
#pragma unroll
    for (int i = 0; i < TM; ++i)
#pragma unroll
      for (int j = 0; j < TN; ++j) {
        int n = n0 + wn * (BN / WN) + j * 16 + fr;
        int which = n / D_;
        int rem = n - which * D_;
        int hh = rem / DH;
        int d = rem - hh * DH;
        int s = m0 + wm * (BM / WM) + i * 16 + fg * 4;
        if (which == 2) {
          int u = (s >> 2) & 3;
          int pos = (s & ~15) + ((((u & 1) << 1) | (u >> 1)) << 2);
          u32x2 v{pk2(acc[i][j][0], acc[i][j][1]), pk2(acc[i][j][2], acc[i][j][3])};
          *(u32x2*)(g.aux3 + (size_t)(hh * 32 + d) * S_ + pos) = v;
        } else {
          u16* dst = (which == 0 ? g.aux : g.aux2) + ((size_t)hh * S_ + s) * 32 + d;
#pragma unroll
          for (int e = 0; e < 4; ++e) dst[e * 32] = f2bf(acc[i][j][e]);
        }
      }
  } else {
    constexpr int CS = BN + 4;
    float* Cs = (float*)smem;
#pragma unroll
    for (int i = 0; i < TM; ++i)
#pragma unroll
      for (int j = 0; j < TN; ++j) {
        int col = wn * (BN / WN) + j * 16 + fr;
        int row = wm * (BM / WM) + i * 16 + fg * 4;
#pragma unroll
        for (int e = 0; e < 4; ++e) Cs[(row + e) * CS + col] = acc[i][j][e];
      }
    __syncthreads();
    constexpr int CH = BM * (BN / 8);
#pragma unroll
    for (int it = 0; it < (CH + NT - 1) / NT; ++it) {
      int id = t + NT * it;
      if (CH % NT == 0 || id < CH) {
        int row = id / (BN / 8), c = id % (BN / 8);
        f32x4 c0 = *(const f32x4*)(Cs + row * CS + c * 8);
        f32x4 c1 = *(const f32x4*)(Cs + row * CS + c * 8 + 4);
        float v[8] = {c0[0], c0[1], c0[2], c0[3], c1[0], c1[1], c1[2], c1[3]};
        size_t go = (size_t)(m0 + row) * N + n0 + c * 8;
        u32x4 o;
        if constexpr (EPI == 1 || EPI == 3) {
          u32x4 r = *(const u32x4*)(g.r1 + go);
#pragma unroll
          for (int e = 0; e < 4; ++e)
            o[e] = pk2(rbf(v[2 * e]) + bflo(r[e]), rbf(v[2 * e + 1]) + bfhi(r[e]));
        } else if constexpr (EPI == 2 || EPI == 4) {
#pragma unroll
          for (int e = 0; e < 4; ++e)
            o[e] = pk2(gelu_tanh(rbf(v[2 * e])), gelu_tanh(rbf(v[2 * e + 1])));
        } else {
          u32x4 r = *(const u32x4*)(g.r1 + go);
          u32x4 r2 = *(const u32x4*)(g.r2 + go);
#pragma unroll
          for (int e = 0; e < 4; ++e) {
            float a = rbf(rbf(v[2 * e]) + bflo(r[e])) + bflo(r2[e]);
            float b = rbf(rbf(v[2 * e + 1]) + bfhi(r[e])) + bfhi(r2[e]);
            o[e] = pk2(a, b);
          }
        }
        *(u32x4*)(g.out + go) = o;
      }
    }
  }
}

__global__ __launch_bounds__(256, 4) void attn_kernel(const u16* __restrict__ Qp, const u16* __restrict__ Kp,
                                                   const u16* __restrict__ Vt, u16* __restrict__ O) {
  __shared__ __attribute__((aligned(16))) char smem[2 * 8192];
  const int t = threadIdx.x;
  const int lane = t & 63, wid = t >> 6;
  const int r = lane & 31, h = lane >> 5;
  const int head = blockIdx.y;
  const int q0 = blockIdx.x * 128 + wid * 32;

  bf16x8 qf[2];
  {
    const u16* qp = Qp + ((size_t)head * S_ + q0 + r) * 32;
    u32x4 a = *(const u32x4*)(qp + 8 * h);
    u32x4 b = *(const u32x4*)(qp + 16 + 8 * h);
    if (h == 0) { b[1] = 0; b[2] = 0; b[3] = 0; }
    else { b = u32x4{0u, 0u, 0u, 0u}; }
    qf[0] = __builtin_bit_cast(bf16x8, a);
    qf[1] = __builtin_bit_cast(bf16x8, b);
  }

  const int kkey = t >> 2, kc = t & 3;
  const int vd = t >> 3, vc = t & 7;
  const u16* kbase = Kp + ((size_t)head * S_ + kkey) * 32 + kc * 8;
  const u16* vbase = Vt + ((size_t)head * 32 + vd) * S_ + vc * 8;
  const int kdst = kkey * 64 + ((kc ^ ((kkey >> 2) & 3)) * 16);
  const int vdst = 4096 + vd * 128 + ((vc ^ ((vd >> 1) & 7)) * 16);

  u32x4 rk, rv;
  auto load_tile = [&](int kt) {
    rk = *(const u32x4*)(kbase + (size_t)kt * 64 * 32);
    if (kc == 2) { rk[1] = 0; rk[2] = 0; rk[3] = 0; }
    if (kc == 3) rk = u32x4{0u, 0u, 0u, 0u};
    if (vd < DH) rv = *(const u32x4*)(vbase + kt * 64);
    else if (vd == DH) rv = u32x4{0x3f803f80u, 0x3f803f80u, 0x3f803f80u, 0x3f803f80u};
    else rv = u32x4{0u, 0u, 0u, 0u};
  };
  auto store_tile = [&](char* buf) {
    *(u32x4*)(buf + kdst) = rk;
    *(u32x4*)(buf + vdst) = rv;
  };

  f32x16 o;
#pragma unroll
  for (int i = 0; i < 16; ++i) o[i] = 0.f;
  float m = -1e30f;
  const float c = 0.23570226039551584f * 1.4426950408889634f;

  load_tile(0);
  store_tile(smem);
  __syncthreads();
  constexpr int NT = S_ / 64;
  for (int kt = 0; kt < NT; ++kt) {
    const char* buf = smem + (kt & 1) * 8192;
    if (kt + 1 < NT) load_tile(kt + 1);
    f32x16 s[2];
#pragma unroll
    for (int sub = 0; sub < 2; ++sub) {
#pragma unroll
      for (int i = 0; i < 16; ++i) s[sub][i] = 0.f;
      int key = sub * 32 + r;
#pragma unroll
      for (int st = 0; st < 2; ++st) {
        int ch = (2 * st + h) ^ ((key >> 2) & 3);
        bf16x8 kf = *(const bf16x8*)(buf + key * 64 + ch * 16);
        s[sub] = __builtin_amdgcn_mfma_f32_32x32x16_bf16(kf, qf[st], s[sub], 0, 0, 0);
      }
    }
    float tm = s[0][0];
#pragma unroll
    for (int i = 1; i < 16; ++i) tm = fmaxf(tm, s[0][i]);
#pragma unroll
    for (int i = 0; i < 16; ++i) tm = fmaxf(tm, s[1][i]);
    tm = fmaxf(tm, __shfl_xor(tm, 32));
    float mn = fmaxf(m, tm);
    float alpha = __builtin_amdgcn_exp2f((m - mn) * c);
    m = mn;
    float mc = mn * c;
#pragma unroll
    for (int i = 0; i < 16; ++i) o[i] *= alpha;
#pragma unroll
    for (int sub = 0; sub < 2; ++sub) {
#pragma unroll
      for (int ks = 0; ks < 2; ++ks) {
        u32x4 p;
#pragma unroll
        for (int e = 0; e < 4; ++e) {
          float a = __builtin_amdgcn_exp2f(fmaf(s[sub][8 * ks + 2 * e], c, -mc));
          float b = __builtin_amdgcn_exp2f(fmaf(s[sub][8 * ks + 2 * e + 1], c, -mc));
          p[e] = pk2(a, b);
        }
        int ch = (2 * (2 * sub + ks) + h) ^ ((r >> 1) & 7);
        bf16x8 vf = *(const bf16x8*)(buf + 4096 + r * 128 + ch * 16);
        o = __builtin_amdgcn_mfma_f32_32x32x16_bf16(vf, __builtin_bit_cast(bf16x8, p), o, 0, 0, 0);
      }
    }
    if (kt + 1 < NT) store_tile(smem + ((kt + 1) & 1) * 8192);
    __syncthreads();
  }
  float l = __shfl(o[10], r);
  float inv = 1.0f / l;
  u16* op = O + (size_t)(q0 + r) * D_ + head * DH;
#pragma unroll
  for (int gq = 0; gq < 3; ++gq) {
    int d0 = 8 * gq + 4 * h;
#pragma unroll
    for (int e = 0; e < 4; ++e) {
      int d = d0 + e;
      if (d < DH) op[d] = f2bf(o[4 * gq + e] * inv);
    }
  }
}

extern "C" void kernel_launch(void* const* d_in, const int* in_sizes, int n_in, void* d_out, int out_size,
                              void* d_ws, size_t ws_size, hipStream_t stream) {
  const u16* x = (const u16*)d_in[0];
  const u16* wqkv = (const u16*)d_in[1];
  const u16* wo = (const u16*)d_in[2];
  const u16* w1 = (const u16*)d_in[4];
  const u16* w2 = (const u16*)d_in[5];
  const u16* w3 = (const u16*)d_in[6];
  const u16* w4 = (const u16*)d_in[7];
  u16* out = (u16*)d_out;

  char* ws = (char*)d_ws;
  size_t off = 0;
  auto alloc = [&](size_t bytes) { char* p = ws + off; off += (bytes + 255) & ~(size_t)255; return p; };
  u16* Qp = (u16*)alloc((size_t)H_ * S_ * 32 * 2);
  u16* Kp = (u16*)alloc((size_t)H_ * S_ * 32 * 2);
  u16* Vt = (u16*)alloc((size_t)H_ * 32 * S_ * 2);
  u16* Ob = (u16*)alloc((size_t)S_ * D_ * 2);
  u16* hb = (u16*)alloc((size_t)S_ * D_ * 2);
  u16* xB = (u16*)alloc((size_t)S_ * D_ * 2);
  u16* hB = (u16*)alloc((size_t)S_ * D_ * 2);
  u16* x1 = (u16*)alloc((size_t)S_ * D_ * 2);
  u16* gb = (u16*)alloc((size_t)S_ * F_ * 2);

  auto smem_bytes = [](int BM, int BN) {
    int raw = BN * 2; int SB = ((raw / 32) & 1) ? raw : raw + 32;
    int buf = BM * 128 + 64 * SB;
    int c = BM * (BN + 4) * 4;
    return (2 * buf > c) ? 2 * buf : c;
  };

  GemmArgs a{};
  a = GemmArgs{x, wqkv, nullptr, nullptr, nullptr, Qp, Kp, Vt, D_, 3 * D_};
  gemm_kernel<64, 96, 4, 2, 1, 0, 9, 9><<<dim3(3 * D_ / 96, S_ / 64), 512, smem_bytes(64, 96), stream>>>(a);
  attn_kernel<<<dim3(S_ / 128, H_), 256, 0, stream>>>(Qp, Kp, Vt, Ob);
  a = GemmArgs{Ob, wo, hb, x, nullptr, nullptr, nullptr, nullptr, D_, D_};
  gemm_kernel<64, 96, 4, 2, 0, 1, 9, 9><<<dim3(D_ / 96, S_ / 64), 512, smem_bytes(64, 96), stream>>>(a);
  a = GemmArgs{hb, w1, hB, nullptr, nullptr, xB, nullptr, nullptr, D_, D_};
  gemm_kernel<64, 96, 4, 2, 2, 2, 9, 9><<<dim3(D_ / 96, S_ / 64), 512, smem_bytes(64, 96), stream>>>(a);
  a = GemmArgs{hB, w2, x1, xB, nullptr, nullptr, nullptr, nullptr, D_, D_};
  gemm_kernel<64, 96, 4, 2, 3, 3, 9, 9><<<dim3(D_ / 96, S_ / 64), 512, smem_bytes(64, 96), stream>>>(a);
  a = GemmArgs{x1, w3, gb, nullptr, nullptr, nullptr, nullptr, nullptr, D_, F_};
  (void)hipFuncSetAttribute((const void*)gemm_kernel<64, 192, 2, 4, 1, 4, 9, 9>, hipFuncAttributeMaxDynamicSharedMemorySize, smem_bytes(64, 192));
  gemm_kernel<64, 192, 2, 4, 1, 4, 9, 9><<<dim3(F_ / 192, S_ / 64), 512, smem_bytes(64, 192), stream>>>(a);
  a = GemmArgs{gb, w4, out, x1, hb, nullptr, nullptr, nullptr, F_, D_};
  gemm_kernel<64, 96, 2, 2, 0, 5, 0, 4><<<dim3(D_ / 96, S_ / 64), 256, smem_bytes(64, 96), stream>>>(a);
}
```

```cpp
#include <hip/hip_runtime.h>
#include <hip/hip_bf16.h>

static constexpr int S_ = 2048;
static constexpr int D_ = 576;
static constexpr int H_ = 32;
static constexpr int DH = 18;
static constexpr int F_ = 4608;

typedef unsigned short u16;
typedef short bf16x8 __attribute__((ext_vector_type(8)));
typedef short s16x4 __attribute__((ext_vector_type(4)));
typedef float f32x4 __attribute__((ext_vector_type(4)));
typedef float f32x16 __attribute__((ext_vector_type(16)));
typedef unsigned u32x4 __attribute__((ext_vector_type(4)));
typedef unsigned u32x2 __attribute__((ext_vector_type(2)));
typedef __attribute__((address_space(3))) s16x4* lds_s16x4_ptr;

#define DI __device__ __forceinline__

DI float bf2f(u16 v) { return __uint_as_float(((unsigned)v) << 16); }
DI float bflo(unsigned u) { return __uint_as_float(u << 16); }
DI float bfhi(unsigned u) { return __uint_as_float(u & 0xffff0000u); }
DI u16 f2bf(float f) { return __builtin_bit_cast(u16, (__bf16)f); }
DI float rbf(float f) { return bf2f(f2bf(f)); }
typedef __bf16 bf16x2_t __attribute__((ext_vector_type(2)));
typedef float f32x2_t __attribute__((ext_vector_type(2)));
DI unsigned pk2(float lo, float hi) {
  f32x2_t v{lo, hi};
  return __builtin_bit_cast(unsigned, __builtin_convertvector(v, bf16x2_t));
}

DI float gelu_tanh(float x) {
  const float k0 = 0.7978845608028654f;
  float u = k0 * (x + 0.044715f * x * x * x);
  float e = __builtin_amdgcn_exp2f(-2.8853900817779268f * u);
  return x * __builtin_amdgcn_rcpf(1.0f + e);
}

DI float sum8(float v) {
  v += __shfl_xor(v, 1);
  v += __shfl_xor(v, 2);
  v += __shfl_xor(v, 4);
  return v;
}

DI float ln_elem(float x, float m, float s) { return rbf(rbf(x - m) / s); }

struct GemmArgs {
  const u16* A;
  const u16* W;
  u16* out;
  const u16* r1;
  const u16* r2;
  u16* aux;
  u16* aux2;
  u16* aux3;
  int K;
  int N;
};

template <int BN>
struct BStride {
  static constexpr int raw = BN * 2;
  static constexpr int t = raw / 32;
  static constexpr int value = (t & 1) ? raw : raw + 32;
};

template <int BM, int BN, int WM, int WN, int PRO, int EPI, int NKC, int PD>
__global__ __launch_bounds__(WM* WN * 64, (WM * WN == 8 && PRO != 2 && BN == 96) ? 4 : 2) void gemm_kernel(GemmArgs g) {
  constexpr int NT = WM * WN * 64;
  constexpr int SB = BStride<BN>::value;
  constexpr int A_BYTES = BM * 128;
  constexpr int B_BYTES = 64 * SB;
  constexpr int BUF = A_BYTES + B_BYTES;
  constexpr int TM = BM / WM / 16;
  constexpr int TN = BN / WN / 16;
  constexpr int A_CH = BM * 8 / NT;
  constexpr int RSTEP = NT / 8;
  constexpr int B_CHUNKS = 64 * (BN / 8);
  constexpr int B_CH = (B_CHUNKS + NT - 1) / NT;
  static_assert(BM * 8 % NT == 0, "A chunks");

  extern __shared__ __attribute__((aligned(16))) char smem[];

  const int t = threadIdx.x;
  const int lane = t & 63;
  const int wid = t >> 6;
  const int wm = wid / WN, wn = wid % WN;
  const int K = g.K, N = g.N;
  const int NK = K / 64 / gridDim.z;
  const int kb_ofs = blockIdx.z * NK;
  const int m0 = blockIdx.y * BM;
  const int n0 = blockIdx.x * BN;
  const int ac = t & 7;

  constexpr int PDB = (NKC == 9) ? ((PRO == 1) ? 2 : 3) : PD;
  u32x4 ra[PD][A_CH];
  u32x4 rb[PDB][B_CH];

  auto load_a = [&](int kb, int slot) {
#pragma unroll
    for (int i = 0; i < A_CH; ++i) {
      int row = (t >> 3) + RSTEP * i;
      int k = (kb + kb_ofs) * 64 + ac * 8;
      if constexpr (PRO == 3) {
        int grp = k / 144;
        int sh = (grp == 0) ? -1 : (grp == 1) ? 1 : (grp == 2) ? -2 : 2;
        int sr = m0 + row + sh;
        if (sr >= 0 && sr < S_) ra[slot][i] = *(const u32x4*)(g.A + (size_t)sr * K + k);
        else ra[slot][i] = u32x4{0u, 0u, 0u, 0u};
      } else {
        ra[slot][i] = *(const u32x4*)(g.A + (size_t)(m0 + row) * K + k);
      }
    }
  };
  auto load_b = [&](int kb, int slot) {
#pragma unroll
    for (int i = 0; i < B_CH; ++i) {
      int id = t + NT * i;
      if (B_CHUNKS % NT == 0 || id < B_CHUNKS) {
        int kr = id / (BN / 8), c = id % (BN / 8);
        rb[slot][i] = *(const u32x4*)(g.W + (size_t)((kb + kb_ofs) * 64 + kr) * N + n0 + c * 8);
      }
    }
  };

  float mA[A_CH], iA[A_CH], mB[A_CH], iB[A_CH];

  auto store_tile = [&](char* buf, int slot, int slotb) {
    char* As = buf;
    char* Bs = buf + A_BYTES;
#pragma unroll
    for (int i = 0; i < A_CH; ++i) {
      int row = (t >> 3) + RSTEP * i;
      u32x4 v = ra[slot][i];
      if constexpr (PRO == 1 || PRO == 2) {
#pragma unroll
        for (int e = 0; e < 4; ++e) {
          float a = rbf(rbf(bflo(v[e]) - mA[i]) * iA[i]), b = rbf(rbf(bfhi(v[e]) - mA[i]) * iA[i]);
          if constexpr (PRO == 2) {
            a = rbf(a - mB[i]) * iB[i];
            b = rbf(b - mB[i]) * iB[i];
          }
          v[e] = pk2(a, b);
        }
      }
      int step = ac >> 2, cc = ac & 3;
      int sw = (row >> 1) & 7;
      int slot_lo = step * 4 + ((2 * cc) & 3);
      int slot_hi = step * 4 + ((2 * cc + 1) & 3);
      int sub = (cc >> 1) * 8;
      *(u32x2*)(As + row * 128 + ((slot_lo ^ sw) * 16) + sub) = u32x2{v[0], v[1]};
      *(u32x2*)(As + row * 128 + ((slot_hi ^ sw) * 16) + sub) = u32x2{v[2], v[3]};
    }
#pragma unroll
    for (int i = 0; i < B_CH; ++i) {
      int id = t + NT * i;
      if (B_CHUNKS % NT == 0 || id < B_CHUNKS) {
        int kr = id / (BN / 8), c = id % (BN / 8);
        *(u32x4*)(Bs + kr * SB + c * 16) = rb[slotb][i];
      }
    }
  };

  f32x4 acc[TM][TN];
#pragma unroll
  for (int i = 0; i < TM; ++i)
#pragma unroll
    for (int j = 0; j < TN; ++j) acc[i][j] = f32x4{0.f, 0.f, 0.f, 0.f};

  const int fr = lane & 15, fg = lane >> 4;
  const int tq = fr >> 2, tp = fr & 3;

  auto compute = [&](const char* buf) {
    const char* As = buf;
    const char* Bs = buf + A_BYTES;
#pragma unroll
    for (int st = 0; st < 2; ++st) {
      bf16x8 af[TM], bfr[TN];
#pragma unroll
      for (int i = 0; i < TM; ++i) {
        int row = wm * (BM / WM) + i * 16 + fr;
        int slot = (st * 4 + fg) ^ ((row >> 1) & 7);
        af[i] = *(const bf16x8*)(As + row * 128 + slot * 16);
      }
#pragma unroll
      for (int j = 0; j < TN; ++j) {
        int col = wn * (BN / WN) + j * 16 + tp * 4;
        const char* p0 = Bs + (st * 32 + fg * 4 + tq) * SB + col * 2;
        s16x4 lo = __builtin_amdgcn_ds_read_tr16_b64_v4i16((lds_s16x4_ptr)(p0));
        s16x4 hi = __builtin_amdgcn_ds_read_tr16_b64_v4i16((lds_s16x4_ptr)(p0 + 16 * SB));
        bfr[j] = __builtin_shufflevector(lo, hi, 0, 1, 2, 3, 4, 5, 6, 7);
      }
#pragma unroll
      for (int i = 0; i < TM; ++i)
#pragma unroll
        for (int j = 0; j < TN; ++j)
          acc[i][j] = __builtin_amdgcn_mfma_f32_16x16x32_bf16(af[i], bfr[j], acc[i][j], 0, 0, 0);
    }
  };

  if constexpr (NKC == 9) {
    static_assert(PD == 9, "pd");
#pragma unroll
    for (int kb = 0; kb < 9; ++kb) load_a(kb, kb);
#pragma unroll
    for (int kb = 0; kb < PDB; ++kb) load_b(kb, kb);
    if constexpr (PRO == 1 || PRO == 2) {
#pragma unroll
      for (int i = 0; i < A_CH; ++i) {
        float s = 0.f;
#pragma unroll
        for (int kb = 0; kb < 9; ++kb)
#pragma unroll
          for (int e = 0; e < 4; ++e) s += bflo(ra[kb][i][e]) + bfhi(ra[kb][i][e]);
        s = sum8(s);
        float mean = s * (1.0f / 576.0f);
        float q = 0.f;
#pragma unroll
        for (int kb = 0; kb < 9; ++kb)
#pragma unroll
          for (int e = 0; e < 4; ++e) {
            float a = bflo(ra[kb][i][e]) - mean, b = bfhi(ra[kb][i][e]) - mean;
            q += a * a + b * b;
          }
        q = sum8(q);
        float var = q * (1.0f / 576.0f);
        float m1 = rbf(mean);
        float i1 = 1.0f / rbf(sqrtf(rbf(rbf(var) + 1e-5f)));
        mA[i] = m1;
        iA[i] = i1;
        if constexpr (PRO == 2) {
          float s2 = 0.f;
          const bool wr = (blockIdx.x == 0);
          u16* xp = g.aux + (size_t)(m0 + (t >> 3) + RSTEP * i) * D_ + ac * 8;
#pragma unroll
          for (int kb = 0; kb < 9; ++kb) {
            u32x4 y;
#pragma unroll
            for (int e = 0; e < 4; ++e) {
              float a = rbf(rbf(bflo(ra[kb][i][e]) - m1) * i1), b = rbf(rbf(bfhi(ra[kb][i][e]) - m1) * i1);
              s2 += a + b;
              y[e] = pk2(a, b);
            }
            if (wr) *(u32x4*)(xp + kb * 64) = y;
          }
          s2 = sum8(s2);
          float mean2 = s2 * (1.0f / 576.0f);
          float q2 = 0.f;
#pragma unroll
          for (int kb = 0; kb < 9; ++kb)
#pragma unroll
            for (int e = 0; e < 4; ++e) {
              float a = rbf(rbf(bflo(ra[kb][i][e]) - m1) * i1) - mean2;
              float b = rbf(rbf(bfhi(ra[kb][i][e]) - m1) * i1) - mean2;
              q2 += a * a + b * b;
            }
          q2 = sum8(q2);
          float var2 = q2 * (1.0f / 576.0f);
          mB[i] = rbf(mean2);
          iB[i] = 1.0f / rbf(sqrtf(rbf(rbf(var2) + 1e-5f)));
        }
      }
    }
    store_tile(smem, 0, 0);
    load_b(PDB, 0);
    __syncthreads();
#pragma unroll
    for (int kb = 0; kb < 9; ++kb) {
      if (kb + 1 < 9) {
        store_tile(smem + ((kb + 1) & 1) * BUF, kb + 1, (kb + 1) % PDB);
        if (kb + 1 + PDB < 9) load_b(kb + 1 + PDB, (kb + 1) % PDB);
      }
      compute(smem + (kb & 1) * BUF);
      __syncthreads();
    }
  } else {
    static_assert(PRO == 0, "ring path: plain A only");
#pragma unroll
    for (int p = 0; p < PD; ++p) { load_a(p, p); load_b(p, p); }
    store_tile(smem, 0, 0);
    load_a(PD, 0); load_b(PD, 0);
    __syncthreads();
    for (int kb0 = 0; kb0 < NK; kb0 += PD) {
#pragma unroll
      for (int p = 0; p < PD; ++p) {
        int kb = kb0 + p;
        if (kb + 1 < NK) {
          store_tile(smem + ((kb + 1) & 1) * BUF, (p + 1) % PD, (p + 1) % PD);
          if (kb + 1 + PD < NK) { load_a(kb + 1 + PD, (p + 1) % PD); load_b(kb + 1 + PD, (p + 1) % PD); }
        }
        compute(smem + (kb & 1) * BUF);
        __syncthreads();
      }
    }
  }

  if constexpr (EPI == 6) {
    float* P = (float*)g.aux + (size_t)blockIdx.z * S_ * N;
#pragma unroll
    for (int i = 0; i < TM; ++i)
#pragma unroll
      for (int j = 0; j < TN; ++j) {
        int n = n0 + wn * (BN / WN) + j * 16 + fr;
        int s = m0 + wm * (BM / WM) + i * 16 + fg * 4;
#pragma unroll
        for (int e = 0; e < 4; ++e) P[(size_t)(s + e) * N + n] = acc[i][j][e];
      }
  } else if constexpr (EPI == 0) {
#pragma unroll
    for (int i = 0; i < TM; ++i)
#pragma unroll
      for (int j = 0; j < TN; ++j) {
        int n = n0 + wn * (BN / WN) + j * 16 + fr;
        int which = n / D_;
        int rem = n - which * D_;
        int hh = rem / DH;
        int d = rem - hh * DH;
        int s = m0 + wm * (BM / WM) + i * 16 + fg * 4;
        if (which == 2) {
          int u = (s >> 2) & 3;
          int pos = (s & ~15) + ((((u & 1) << 1) | (u >> 1)) << 2);
          u32x2 v{pk2(acc[i][j][0], acc[i][j][1]), pk2(acc[i][j][2], acc[i][j][3])};
          *(u32x2*)(g.aux3 + (size_t)(hh * 32 + d) * S_ + pos) = v;
        } else {
          u16* dst = (which == 0 ? g.aux : g.aux2) + ((size_t)hh * S_ + s) * 32 + d;
#pragma unroll
          for (int e = 0; e < 4; ++e) dst[e * 32] = f2bf(acc[i][j][e]);
        }
      }
  } else {
    constexpr int CS = BN + 4;
    float* Cs = (float*)smem;
#pragma unroll
    for (int i = 0; i < TM; ++i)
#pragma unroll
      for (int j = 0; j < TN; ++j) {
        int col = wn * (BN / WN) + j * 16 + fr;
        int row = wm * (BM / WM) + i * 16 + fg * 4;
#pragma unroll
        for (int e = 0; e < 4; ++e) Cs[(row + e) * CS + col] = acc[i][j][e];
      }
    __syncthreads();
    constexpr int CH = BM * (BN / 8);
#pragma unroll
    for (int it = 0; it < (CH + NT - 1) / NT; ++it) {
      int id = t + NT * it;
      if (CH % NT == 0 || id < CH) {
        int row = id / (BN / 8), c = id % (BN / 8);
        f32x4 c0 = *(const f32x4*)(Cs + row * CS + c * 8);
        f32x4 c1 = *(const f32x4*)(Cs + row * CS + c * 8 + 4);
        float v[8] = {c0[0], c0[1], c0[2], c0[3], c1[0], c1[1], c1[2], c1[3]};
        size_t go = (size_t)(m0 + row) * N + n0 + c * 8;
        u32x4 o;
        if constexpr (EPI == 1 || EPI == 3) {
          u32x4 r = *(const u32x4*)(g.r1 + go);
#pragma unroll
          for (int e = 0; e < 4; ++e)
            o[e] = pk2(rbf(v[2 * e]) + bflo(r[e]), rbf(v[2 * e + 1]) + bfhi(r[e]));
        } else if constexpr (EPI == 2 || EPI == 4) {
#pragma unroll
          for (int e = 0; e < 4; ++e)
            o[e] = pk2(gelu_tanh(rbf(v[2 * e])), gelu_tanh(rbf(v[2 * e + 1])));
        } else {
          u32x4 r = *(const u32x4*)(g.r1 + go);
          u32x4 r2 = *(const u32x4*)(g.r2 + go);
#pragma unroll
          for (int e = 0; e < 4; ++e) {
            float a = rbf(rbf(v[2 * e]) + bflo(r[e])) + bflo(r2[e]);
            float b = rbf(rbf(v[2 * e + 1]) + bfhi(r[e])) + bfhi(r2[e]);
            o[e] = pk2(a, b);
          }
        }
        *(u32x4*)(g.out + go) = o;
      }
    }
  }
}

__global__ __launch_bounds__(256) void reduce_kernel(const float* __restrict__ P, const u16* __restrict__ r1,
                                                     const u16* __restrict__ r2, u16* __restrict__ out) {
  size_t i = ((size_t)blockIdx.x * 256 + threadIdx.x) * 8;
  const size_t SL = (size_t)S_ * D_;
  f32x4 a0 = *(const f32x4*)(P + i), a1 = *(const f32x4*)(P + i + 4);
#pragma unroll
  for (int z = 1; z < 4; ++z) {
    a0 += *(const f32x4*)(P + z * SL + i);
    a1 += *(const f32x4*)(P + z * SL + i + 4);
  }
  float v[8] = {a0[0], a0[1], a0[2], a0[3], a1[0], a1[1], a1[2], a1[3]};
  u32x4 r = *(const u32x4*)(r1 + i), q = *(const u32x4*)(r2 + i), o;
#pragma unroll
  for (int e = 0; e < 4; ++e) {
    float a = rbf(rbf(v[2 * e]) + bflo(r[e])) + bflo(q[e]);
    float b = rbf(rbf(v[2 * e + 1]) + bfhi(r[e])) + bfhi(q[e]);
    o[e] = pk2(a, b);
  }
  *(u32x4*)(out + i) = o;
}

__global__ __launch_bounds__(256, 4) void attn_kernel(const u16* __restrict__ Qp, const u16* __restrict__ Kp,
                                                   const u16* __restrict__ Vt, u16* __restrict__ O) {
  __shared__ __attribute__((aligned(16))) char smem[2 * 8192];
  const int t = threadIdx.x;
  const int lane = t & 63, wid = t >> 6;
  const int r = lane & 31, h = lane >> 5;
  const int head = blockIdx.y;
  const int q0 = blockIdx.x * 128 + wid * 32;

  bf16x8 qf[2];
  {
    const u16* qp = Qp + ((size_t)head * S_ + q0 + r) * 32;
    u32x4 a = *(const u32x4*)(qp + 8 * h);
    u32x4 b = *(const u32x4*)(qp + 16 + 8 * h);
    if (h == 0) { b[1] = 0; b[2] = 0; b[3] = 0; }
    else { b = u32x4{0u, 0u, 0u, 0u}; }
    qf[0] = __builtin_bit_cast(bf16x8, a);
    qf[1] = __builtin_bit_cast(bf16x8, b);
  }

  const int kkey = t >> 2, kc = t & 3;
  const int vd = t >> 3, vc = t & 7;
  const u16* kbase = Kp + ((size_t)head * S_ + kkey) * 32 + kc * 8;
  const u16* vbase = Vt + ((size_t)head * 32 + vd) * S_ + vc * 8;
  const int kdst = kkey * 64 + ((kc ^ ((kkey >> 2) & 3)) * 16);
  const int vdst = 4096 + vd * 128 + ((vc ^ ((vd >> 1) & 7)) * 16);

  u32x4 rk, rv;
  auto load_tile = [&](int kt) {
    rk = *(const u32x4*)(kbase + (size_t)kt * 64 * 32);
    if (kc == 2) { rk[1] = 0; rk[2] = 0; rk[3] = 0; }
    if (kc == 3) rk = u32x4{0u, 0u, 0u, 0u};
    if (vd < DH) rv = *(const u32x4*)(vbase + kt * 64);
    else if (vd == DH) rv = u32x4{0x3f803f80u, 0x3f803f80u, 0x3f803f80u, 0x3f803f80u};
    else rv = u32x4{0u, 0u, 0u, 0u};
  };
  auto store_tile = [&](char* buf) {
    *(u32x4*)(buf + kdst) = rk;
    *(u32x4*)(buf + vdst) = rv;
  };

  f32x16 o;
#pragma unroll
  for (int i = 0; i < 16; ++i) o[i] = 0.f;
  float m = -1e30f;
  const float c = 0.23570226039551584f * 1.4426950408889634f;

  load_tile(0);
  store_tile(smem);
  __syncthreads();
  constexpr int NT = S_ / 64;
  for (int kt = 0; kt < NT; ++kt) {
    const char* buf = smem + (kt & 1) * 8192;
    if (kt + 1 < NT) load_tile(kt + 1);
    f32x16 s[2];
#pragma unroll
    for (int sub = 0; sub < 2; ++sub) {
#pragma unroll
      for (int i = 0; i < 16; ++i) s[sub][i] = 0.f;
      int key = sub * 32 + r;
#pragma unroll
      for (int st = 0; st < 2; ++st) {
        int ch = (2 * st + h) ^ ((key >> 2) & 3);
        bf16x8 kf = *(const bf16x8*)(buf + key * 64 + ch * 16);
        s[sub] = __builtin_amdgcn_mfma_f32_32x32x16_bf16(kf, qf[st], s[sub], 0, 0, 0);
      }
    }
    float tm = s[0][0];
#pragma unroll
    for (int i = 1; i < 16; ++i) tm = fmaxf(tm, s[0][i]);
#pragma unroll
    for (int i = 0; i < 16; ++i) tm = fmaxf(tm, s[1][i]);
    tm = fmaxf(tm, __shfl_xor(tm, 32));
    float mn = fmaxf(m, tm);
    float alpha = __builtin_amdgcn_exp2f((m - mn) * c);
    m = mn;
    float mc = mn * c;
#pragma unroll
    for (int i = 0; i < 16; ++i) o[i] *= alpha;
#pragma unroll
    for (int sub = 0; sub < 2; ++sub) {
#pragma unroll
      for (int ks = 0; ks < 2; ++ks) {
        u32x4 p;
#pragma unroll
        for (int e = 0; e < 4; ++e) {
          float a = __builtin_amdgcn_exp2f(fmaf(s[sub][8 * ks + 2 * e], c, -mc));
          float b = __builtin_amdgcn_exp2f(fmaf(s[sub][8 * ks + 2 * e + 1], c, -mc));
          p[e] = pk2(a, b);
        }
        int ch = (2 * (2 * sub + ks) + h) ^ ((r >> 1) & 7);
        bf16x8 vf = *(const bf16x8*)(buf + 4096 + r * 128 + ch * 16);
        o = __builtin_amdgcn_mfma_f32_32x32x16_bf16(vf, __builtin_bit_cast(bf16x8, p), o, 0, 0, 0);
      }
    }
    if (kt + 1 < NT) store_tile(smem + ((kt + 1) & 1) * 8192);
    __syncthreads();
  }
  float l = __shfl(o[10], r);
  float inv = 1.0f / l;
  u16* op = O + (size_t)(q0 + r) * D_ + head * DH;
#pragma unroll
  for (int gq = 0; gq < 3; ++gq) {
    int d0 = 8 * gq + 4 * h;
#pragma unroll
    for (int e = 0; e < 4; ++e) {
      int d = d0 + e;
      if (d < DH) op[d] = f2bf(o[4 * gq + e] * inv);
    }
  }
}

extern "C" void kernel_launch(void* const* d_in, const int* in_sizes, int n_in, void* d_out, int out_size,
                              void* d_ws, size_t ws_size, hipStream_t stream) {
  const u16* x = (const u16*)d_in[0];
  const u16* wqkv = (const u16*)d_in[1];
  const u16* wo = (const u16*)d_in[2];
  const u16* w1 = (const u16*)d_in[4];
  const u16* w2 = (const u16*)d_in[5];
  const u16* w3 = (const u16*)d_in[6];
  const u16* w4 = (const u16*)d_in[7];
  u16* out = (u16*)d_out;

  char* ws = (char*)d_ws;
  size_t off = 0;
  auto alloc = [&](size_t bytes) { char* p = ws + off; off += (bytes + 255) & ~(size_t)255; return p; };
  u16* Qp = (u16*)alloc((size_t)H_ * S_ * 32 * 2);
  u16* Kp = (u16*)alloc((size_t)H_ * S_ * 32 * 2);
  u16* Vt = (u16*)alloc((size_t)H_ * 32 * S_ * 2);
  u16* Ob = (u16*)alloc((size_t)S_ * D_ * 2);
  u16* hb = (u16*)alloc((size_t)S_ * D_ * 2);
  u16* xB = (u16*)alloc((size_t)S_ * D_ * 2);
  u16* hB = (u16*)alloc((size_t)S_ * D_ * 2);
  u16* x1 = (u16*)alloc((size_t)S_ * D_ * 2);
  u16* gb = (u16*)alloc((size_t)S_ * F_ * 2);

  auto smem_bytes = [](int BM, int BN) {
    int raw = BN * 2; int SB = ((raw / 32) & 1) ? raw : raw + 32;
    int buf = BM * 128 + 64 * SB;
    int c = BM * (BN + 4) * 4;
    return (2 * buf > c) ? 2 * buf : c;
  };

  GemmArgs a{};
  a = GemmArgs{x, wqkv, nullptr, nullptr, nullptr, Qp, Kp, Vt, D_, 3 * D_};
  gemm_kernel<64, 96, 4, 2, 1, 0, 9, 9><<<dim3(3 * D_ / 96, S_ / 64), 512, smem_bytes(64, 96), stream>>>(a);
  attn_kernel<<<dim3(S_ / 128, H_), 256, 0, stream>>>(Qp, Kp, Vt, Ob);
  a = GemmArgs{Ob, wo, hb, x, nullptr, nullptr, nullptr, nullptr, D_, D_};
  gemm_kernel<64, 96, 4, 2, 0, 1, 9, 9><<<dim3(D_ / 96, S_ / 64), 512, smem_bytes(64, 96), stream>>>(a);
  a = GemmArgs{hb, w1, hB, nullptr, nullptr, xB, nullptr, nullptr, D_, D_};
  gemm_kernel<64, 96, 4, 2, 2, 2, 9, 9><<<dim3(D_ / 96, S_ / 64), 512, smem_bytes(64, 96), stream>>>(a);
  a = GemmArgs{hB, w2, x1, xB, nullptr, nullptr, nullptr, nullptr, D_, D_};
  gemm_kernel<64, 96, 4, 2, 3, 3, 9, 9><<<dim3(D_ / 96, S_ / 64), 512, smem_bytes(64, 96), stream>>>(a);
  a = GemmArgs{x1, w3, gb, nullptr, nullptr, nullptr, nullptr, nullptr, D_, F_};
  (void)hipFuncSetAttribute((const void*)gemm_kernel<64, 192, 2, 4, 1, 4, 9, 9>, hipFuncAttributeMaxDynamicSharedMemorySize, smem_bytes(64, 192));
  gemm_kernel<64, 192, 2, 4, 1, 4, 9, 9><<<dim3(F_ / 192, S_ / 64), 512, smem_bytes(64, 192), stream>>>(a);
  float* part = (float*)alloc((size_t)4 * S_ * D_ * 4);
  a = GemmArgs{gb, w4, nullptr, nullptr, nullptr, (u16*)part, nullptr, nullptr, F_, D_};
  gemm_kernel<64, 96, 2, 2, 0, 6, 0, 3><<<dim3(D_ / 96, S_ / 64, 4), 256, smem_bytes(64, 96), stream>>>(a);
  reduce_kernel<<<dim3(S_ * D_ / 8 / 256), 256, 0, stream>>>(part, x1, hb, out);
}
```
